```python
import torch

def _softmax(x, axis=-1):
    return torch.softmax(x, dim=axis)

def _gelu(x):
    return torch.nn.functional.gelu(x, approximate='tanh')

def _silu(x):
    return torch.nn.functional.silu(x)

def _sigmoid(x):
    return torch.sigmoid(x)

def _layernorm(x, axis=-1, eps=1e-05):
    m = x.mean(dim=axis, keepdim=True)
    v = x.var(dim=axis, keepdim=True, unbiased=False)
    return (x - m) / torch.sqrt(v + eps)

def _scatter_add(tgt, idx, src, axis=0):
    return tgt.clone().index_add_(axis, idx, src)

def _segment_sum(src, seg, n):
    out = torch.zeros((n,) + src.shape[1:], dtype=src.dtype)
    return out.index_add_(0, seg, src)

def _pad(x, pw):
    flat = tuple((v for pair in reversed(pw) for v in pair))
    return torch.nn.functional.pad(x, flat)
kernel_name = 'adaLN_cond__sliding_block_cached__swiglu_ffn__decode'

def setup_inputs():

    _w = lambda a: (lambda t: t.to(torch.bfloat16) if t.is_floating_point() else t)(torch.as_tensor(a))

    def _A_setup():
        B, Sq, D = (1, 1, 1024)
        Hq, Hkv, Sk, Dh = (32, 4, 65536, 128)
        assert Hq % Hkv == 0, (Hq, Hkv)
        return {'x': _w((lambda *s: torch.randn(*s, dtype=torch.bfloat16) * 0.1)(B, Sq, D)), 'k_cache': _w((lambda *s: torch.randn(*s, dtype=torch.bfloat16) * 0.8)(B, Hkv, Sk, Dh)), 'v_cache': _w((lambda *s: torch.randn(*s, dtype=torch.bfloat16) * 0.8)(B, Hkv, Sk, Dh)), 'wq': _w((lambda *s: torch.randn(*s, dtype=torch.bfloat16) * 0.8)(D, Hq * Dh) * D ** (-0.5)), 'wo': _w((lambda *s: torch.randn(*s, dtype=torch.bfloat16) * 0.8)(Hq * Dh, D) * (Hq * Dh) ** (-0.5))}
    _A_in = _A_setup()

    def _B_setup():
        B, S, D, F = (1, 1, 1024, 3584)
        return {'x': _w((lambda *s: torch.randn(*s, dtype=torch.bfloat16) * 0.1)(B, S, D)), 'w_gate': _w((lambda *s: torch.randn(*s, dtype=torch.bfloat16) * 0.1)(D, F)), 'w_up': _w((lambda *s: torch.randn(*s, dtype=torch.bfloat16) * 0.1)(D, F)), 'w_down': _w((lambda *s: torch.randn(*s, dtype=torch.bfloat16) * 0.1)(F, D))}
    _B_in = _B_setup()
    return {'A_x': _A_in['x'], 'A_k_cache': _A_in['k_cache'], 'A_v_cache': _A_in['v_cache'], 'A_wq': _A_in['wq'], 'A_wo': _A_in['wo'], 'B_x': _B_in['x'], 'B_w_gate': _B_in['w_gate'], 'B_w_up': _B_in['w_up'], 'B_w_down': _B_in['w_down'], 'c': _w((lambda *s: torch.randn(*s, dtype=torch.bfloat16) * 0.2)(1, 1024) * 8.0), 'w_mod': _w((lambda *s: torch.randn(*s, dtype=torch.bfloat16) * 0.2)(1024, 6 * 1024) * 1024 ** (-0.5) * 8.0)}

def reference(A_x, A_k_cache, A_v_cache, A_wq, A_wo, B_x, B_w_gate, B_w_up, B_w_down, c, w_mod):

    def _A_ref(x, k_cache, v_cache, wq, wo):
        B, Sq, D = x.shape
        Hq, Dh = (32, 128)
        Hkv, Sk = (k_cache.shape[1], k_cache.shape[2])
        W = min(4096, max(256, Sk // 4))
        rep = Hq // Hkv
        q = (lambda x, *d: x.permute(*d))((x @ wq).reshape(B, Sq, Hq, Dh), 0, 2, 1, 3)
        k = (lambda x, n, axis: torch.repeat_interleave(x, n, dim=axis))(k_cache[:, :, Sk - W:, :], rep, axis=1)
        v = (lambda x, n, axis: torch.repeat_interleave(x, n, dim=axis))(v_cache[:, :, Sk - W:, :], rep, axis=1)
        scores = torch.einsum('bhqd,bhkd->bhqk', q, k) * Dh ** (-0.5)
        p = _softmax(scores, axis=-1)
        o = torch.einsum('bhqk,bhkd->bhqd', p, v)
        return (lambda x, *d: x.permute(*d))(o, 0, 2, 1, 3).reshape(B, Sq, Hq * Dh) @ wo

    def _B_ref(x, w_gate, w_up, w_down):
        g = _silu(x @ w_gate)
        u = x @ w_up
        return g * u @ w_down
    x = A_x
    mod = (_silu(c) @ w_mod).reshape(c.shape[0], 6, 1, -1)
    sa, ca, ga, sb, cb, gb = (mod[:, 0].clone(), mod[:, 1].clone(), mod[:, 2].clone(), mod[:, 3].clone(), mod[:, 4].clone(), mod[:, 5].clone())
    h = _layernorm(x, axis=-1) * (1 + ca) + sa
    x = x + ga * _A_ref(h, A_k_cache, A_v_cache, A_wq, A_wo)
    h = _layernorm(x, axis=-1) * (1 + cb) + sb
    x = x + gb * _B_ref(h, B_w_gate, B_w_up, B_w_down)
    return x
```

```cpp
#include <hip/hip_runtime.h>
#include <stdint.h>

typedef unsigned short u16;
typedef unsigned long long u64;
typedef unsigned v4u __attribute__((ext_vector_type(4)));
typedef unsigned v2u __attribute__((ext_vector_type(2)));
typedef float f4 __attribute__((ext_vector_type(4)));
typedef float f2 __attribute__((ext_vector_type(2)));
typedef __bf16 bf2 __attribute__((ext_vector_type(2)));
typedef __bf16 bf8 __attribute__((ext_vector_type(8)));
typedef short s4 __attribute__((ext_vector_type(4)));
#define LAS __attribute__((address_space(3)))
typedef __amdgpu_buffer_rsrc_t rsrc_t;

constexpr int SK = 65536, WIN = 4096, FF = 3584;
constexpr int CODE_KB = 15, DLY_KV = 30, DLY_S = 270, DLY_D = 90, NPRE = 2, W_G = 5, MC = 6;
constexpr int WS_EPOCH = 0;
constexpr int WS_M1 = 4096;
constexpr int WS_Q = WS_M1 + 8 * 2048 * 4;
constexpr int WS_SO = WS_Q + 4 * 4096 * 4;
constexpr int WS_O = WS_SO + 4 * 32 * 4096;
constexpr int WS_M2 = WS_O + 32 * 1024 * 4;
constexpr int WS_G = WS_M2 + 8 * 4096 * 4;
constexpr int WS_ST = WS_G + 8 * 3 * 7168 * 4;
constexpr int WS_D = WS_ST + 14 * 8 * 128;
constexpr int WS_END = WS_D + 28 * 1024 * 4;
constexpr int L_RED = 0;
constexpr int L_V = 32768;
constexpr int L_Q = 65536;
constexpr int L_P = L_Q + 8 * 288;
constexpr int L_MX = L_P + 8 * 288;
constexpr int L_K = 73728;
constexpr int LDS_BYTES = 104 * 1024;
constexpr int AUX_NT = 2, AUX_SC1 = 16;
constexpr int SPIN_CAP = 1 << 16;
constexpr int OOB = 0x7fffff00;

__device__ __forceinline__ float asf(unsigned u) { return __builtin_bit_cast(float, u); }
__device__ __forceinline__ unsigned asu(float f) { return __builtin_bit_cast(unsigned, f); }
__device__ __forceinline__ unsigned pk2(float lo, float hi) { f2 p = {lo, hi}; return __builtin_bit_cast(unsigned, __builtin_convertvector(p, bf2)); }
__device__ __forceinline__ float lo16(unsigned w) { return asf(w << 16); }
__device__ __forceinline__ float hi16(unsigned w) { return asf(w & 0xffff0000u); }
__device__ __forceinline__ float rbf(float f) { return hi16(pk2(f, f)); }
template <int C> __device__ __forceinline__ unsigned dppu(unsigned x) { return (unsigned)__builtin_amdgcn_update_dpp(0, (int)x, C, 0xf, 0xf, true); }
template <int C> __device__ __forceinline__ float dppf(float x) { return asf(dppu<C>(asu(x))); }
constexpr int XOR1 = 0xB1, XOR2 = 0x4E, HMIRROR = 0x141, ROR4 = 0x124, ROR8 = 0x128;
#define BCAST(n) (0x150 + (n))
#define QBC(n) ((n) * 0x55)
__device__ __forceinline__ float x4_sum(float a) {
    float r;
    asm("s_nop 1\n\tv_add_f32_dpp %0, %1, %1 row_shl:4 row_mask:0xf bank_mask:0x5\n\tv_add_f32_dpp %0, %1, %1 row_shr:4 row_mask:0xf bank_mask:0xa" : "=&v"(r) : "v"(a));
    return r;
}
__device__ __forceinline__ float tp32(float a, float b) { auto s = __builtin_amdgcn_permlane32_swap(asu(a), asu(b), false, false); return asf(s[0]) + asf(s[1]); }
__device__ __forceinline__ float tp16(float a, float b) { auto s = __builtin_amdgcn_permlane16_swap(asu(a), asu(b), false, false); return asf(s[0]) + asf(s[1]); }
__device__ __forceinline__ float tp4(f4 v) { return tp16(tp32(v.x, v.z), tp32(v.y, v.w)); }
__device__ __forceinline__ float q_sum(float t) { t += dppf<ROR4>(t); t += dppf<ROR8>(t); return t; }
__device__ __forceinline__ float mx(float a, float b) { float r; asm("v_max_f32 %0, %1, %2" : "=v"(r) : "v"(a), "v"(b)); return r; }
__device__ __forceinline__ float mx3(float a, float b, float c) { float r; asm("v_max3_f32 %0, %1, %2, %3" : "=v"(r) : "v"(a), "v"(b), "v"(c)); return r; }
__device__ __forceinline__ float x16_sum(float x) { return tp16(x, x); }
__device__ __forceinline__ float x32_sum(float x) { return tp32(x, x); }
__device__ __forceinline__ float x16_max(float x) { auto s = __builtin_amdgcn_permlane16_swap(asu(x), asu(x), false, false); return mx(asf(s[0]), asf(s[1])); }
__device__ __forceinline__ float x32_max(float x) { auto s = __builtin_amdgcn_permlane32_swap(asu(x), asu(x), false, false); return mx(asf(s[0]), asf(s[1])); }
__device__ __forceinline__ float wave_sum(float x) { x += dppf<XOR1>(x); x += dppf<XOR2>(x); x += dppf<HMIRROR>(x); x += dppf<ROR8>(x); return x32_sum(x16_sum(x)); }
__device__ __forceinline__ f4 x32_sum4(f4 s) { return f4{x32_sum(s.x), x32_sum(s.y), x32_sum(s.z), x32_sum(s.w)}; }
__device__ __forceinline__ f4 x16_sum4(f4 s) { return f4{x16_sum(s.x), x16_sum(s.y), x16_sum(s.z), x16_sum(s.w)}; }
__device__ __forceinline__ f4 mfma(v4u a, v4u b, f4 c) { return __builtin_amdgcn_mfma_f32_16x16x32_bf16(__builtin_bit_cast(bf8, a), __builtin_bit_cast(bf8, b), c, 0, 0, 0); }
__device__ __forceinline__ rsrc_t mk(const void* p, int bytes) { return __builtin_amdgcn_make_buffer_rsrc((void*)p, 0, bytes, 0x00020000); }
__device__ __forceinline__ v4u ldw(rsrc_t r, int voff, int soff) { return __builtin_amdgcn_raw_buffer_load_b128(r, voff, soff, AUX_NT); }
__device__ __forceinline__ v4u ldh(rsrc_t r, int voff, int soff) { return __builtin_amdgcn_raw_buffer_load_b128(r, voff, soff, AUX_SC1); }
__device__ __forceinline__ unsigned ldh1(rsrc_t r, int voff, int soff) { return __builtin_amdgcn_raw_buffer_load_b32(r, voff, soff, AUX_SC1); }
__device__ __forceinline__ unsigned ld16(rsrc_t r, int voff) { return __builtin_amdgcn_raw_buffer_load_b16(r, voff, 0, 0); }
__device__ __forceinline__ unsigned tg(float f, unsigned T) { return __builtin_amdgcn_perm(asu(f), T, 0x07060500u); }
__device__ __forceinline__ void pub4(rsrc_t r, int voff, f4 v, unsigned T) { v4u o = {tg(v.x, T), tg(v.y, T), tg(v.z, T), tg(v.w, T)}; __builtin_amdgcn_raw_buffer_store_b128(o, r, voff, 0, AUX_SC1); }
__device__ __forceinline__ unsigned ne3(unsigned a, unsigned b, unsigned t) { return __builtin_amdgcn_bitop3_b32(a, b, t, 0x7e); }
__device__ __forceinline__ unsigned df4(v4u v, unsigned T) { return ne3(v.x, v.y, T) | ne3(v.z, v.w, T); }
__device__ __forceinline__ bool all_tagged(unsigned d) { return __builtin_amdgcn_ballot_w64((d & 0xffu) != 0u) == 0ull; }
__device__ __forceinline__ float ut(unsigned v) { return asf(v & 0xffffff00u); }
__device__ __forceinline__ f4 ut4(v4u v) { return f4{ut(v.x), ut(v.y), ut(v.z), ut(v.w)}; }
#define SB() __builtin_amdgcn_sched_barrier(0)
#define PIN(x) asm volatile("" : "+v"(x))
#define BAR() do { asm volatile("s_waitcnt lgkmcnt(0)" ::: "memory"); __builtin_amdgcn_s_barrier(); asm volatile("" ::: "memory"); } while (0)
#define ORD() do { SB(); __builtin_amdgcn_s_barrier(); SB(); } while (0)
#define PACE(k, W) do { if ((k) >= (W)) asm volatile("s_waitcnt vmcnt(%0)" ::"n"((W) - 1) : "memory"); SB(); } while (0)
#define POLL_PAUSE() asm volatile("s_sleep 2" ::: "memory")
#define SPIN _Pragma("nounroll") for (int spin = 0; spin < SPIN_CAP; ++spin)
template <int C> __device__ __forceinline__ v4u amk(unsigned X, v4u m) { const unsigned b = dppu<C>(X); return v4u{b & m.x, b & m.y, b & m.z, b & m.w}; }
__device__ __forceinline__ float silu(float v) { return v * __builtin_amdgcn_rcpf(1.f + __builtin_amdgcn_exp2f(v * -1.44269504088896f)); }
__device__ __forceinline__ u64 now() { return __builtin_amdgcn_s_memrealtime(); }
__device__ __forceinline__ void wait_until(u64 t) { SPIN { if ((long long)(now() - t) >= 0) break; asm volatile("s_sleep 1" ::: "memory"); } }
__device__ __forceinline__ void sentinel(rsrc_t ws, int voff, int soff, unsigned T) {
    const int o = __builtin_amdgcn_readfirstlane(voff);
    SPIN {
        const unsigned v = ldh1(ws, o, soff);
        if (((__builtin_amdgcn_readfirstlane(v) ^ T) & 0xffu) == 0u) break;
        POLL_PAUSE();
    }
}

__global__ void __launch_bounds__(512, 2) fused_block(const u16* __restrict__ px, const u16* __restrict__ pk, const u16* __restrict__ pv, const u16* __restrict__ pwq,
                                                   const u16* __restrict__ pwo, const u16* __restrict__ pwg, const u16* __restrict__ pwu, const u16* __restrict__ pwd,
                                                   const u16* __restrict__ pc, const u16* __restrict__ pwm, u16* __restrict__ pout, char* __restrict__ pws) {
    __shared__ __attribute__((aligned(16))) char lds_raw[LDS_BYTES];
    LAS char* const L = (LAS char*)lds_raw;
    const int tid = threadIdx.x, lane = tid & 63, wave = __builtin_amdgcn_readfirstlane(tid >> 6);
    const int b = blockIdx.x, xc = b & 7, ii = b >> 3, ib = ii - 16;
    const int c16 = lane & 15, g = lane >> 4;
    const int m1row = (b >> 5) * 128 + wave * 16 + 2 * g + (c16 >> 3);
    int m1off = m1row * 12288 + ((b & 31) * 64 + (c16 & 7) * 8) * 2, ccoff = (m1row + 8 * (lane & 1)) * 2;
    PIN(m1off); PIN(ccoff);
    SB();
    asm volatile("" ::"s"(px), "s"(pk), "s"(pv), "s"(pwq), "s"(pwo), "s"(pwg), "s"(pwu), "s"(pwd), "s"(pc), "s"(pwm), "s"(pout), "s"(pws));
    const rsrc_t ws = mk(pws, WS_END), rm = mk(pwm, 1024 * 6144 * 2), rc = mk(pc, 2048), rx = mk(px, 2048);
    const v4u m0 = ldw(rm, m1off, 0), m1 = ldw(rm, m1off, 8 * 12288);
    const unsigned cc = ld16(rc, ccoff);
    const unsigned ep = ldh1(ws, WS_EPOCH, 0);
    SB();
    u64 pc0;
    asm volatile("s_getpc_b64 %0" : "=s"(pc0));
    const int co = ib & 3, cm2 = ib ^ 1;
    const bool roleA = b < 128;
    const f4 zero4 = {0.f, 0.f, 0.f, 0.f};
    v4u wA[8], wB[8], wC[8], msk;
    unsigned T, E1;
    const rsrc_t rq = mk(pwq, roleA ? 0 : 1024 * 4096 * 2);
    const int vq = ((xc >> 1) * 256 + wave * 32 + g) * 8192 + ((ib * 2 + (xc & 1)) * 128 + c16 * 8) * 2;

    {
        const v4u pf = __builtin_amdgcn_raw_buffer_load_b128(mk((const void*)(pc0 & ~255ull), wave == 7 ? CODE_KB * 1024 : 0), lane * 16, (ii & 15) * 1024, 0);
        ORD();
#pragma unroll
        for (int f = 0; f < 4; ++f) wA[f] = ldw(rq, vq, f * 32768);
        SB();
        const int e = lane & 7;
        const unsigned hm = (e & 1) ? 0xffff0000u : 0x0000ffffu;
        msk = v4u{(e >> 1) == 0 ? hm : 0u, (e >> 1) == 1 ? hm : 0u, (e >> 1) == 2 ? hm : 0u, (e >> 1) == 3 ? hm : 0u};
        int lw = L_RED + ((wave * 2 + (g >> 1)) * 64 + (c16 & 7) * 8 + (g & 1) * 4) * 4, lr = L_RED + (g * 4 * 64 + c16 * 4) * 4;
        int po = lane < 16 ? WS_M1 + ((b >> 5) * 2048 + (b & 31) * 64 + lane * 4) * 4 : OOB;
        PIN(msk); PIN(lw); PIN(lr); PIN(po);
        const float sc = silu(asf(cc << 16));
        const unsigned XC = pk2(sc, sc);
        v4u A0 = amk<QBC(0)>(XC, msk), A1 = amk<QBC(1)>(XC, msk);
        E1 = __builtin_amdgcn_readfirstlane(ep) + 1u;
        T = E1 & 0xffu;
        PIN(A0); PIN(A1);
        const f4 acc = mfma(A1, m1, mfma(A0, m0, zero4));
        if ((c16 >> 3) == (g >> 1)) *(LAS f4*)(L + lw) = acc;
        BAR();
        if (wave == 0) {
            const LAS f4* r = (const LAS f4*)(L + lr);
            pub4(ws, po, x32_sum4(x16_sum4((r[0] + r[16]) + (r[32] + r[48]))), T);
        }
        asm volatile("" ::"v"(pf));
        ORD();
    }
    auto sum8 = [&](int lr, int stride) {
        const LAS f4* r = (const LAS f4*)(L + lr);
        const f4 a = r[0], b_ = r[stride], c = r[2 * stride], d = r[3 * stride], e = r[4 * stride], f = r[5 * stride], g_ = r[6 * stride], h = r[7 * stride];
        return ((a + b_) + (c + d)) + ((e + f) + (g_ + h));
    };
    auto reduce256 = [&](f4 a0, f4 a1, int lw, int lr, int po) {
        if (lane < 32) { LAS f4* r = (LAS f4*)(L + lw); r[0] = a0; r[32] = a1; }
        BAR();
        if (wave == 0) pub4(ws, po, sum8(lr, 64), T);
    };
    const int lw256 = (wave * 256 + c16 * 8 + g * 4) * 4, lr256 = lane * 16, po256 = lane * 16;
    auto x_mean = [&](v4u x0, v4u x1, float (&v)[16]) {
        const float t[16] = {lo16(x0.x), hi16(x0.x), lo16(x0.y), hi16(x0.y), lo16(x0.z), hi16(x0.z), lo16(x0.w), hi16(x0.w),
                             lo16(x1.x), hi16(x1.x), lo16(x1.y), hi16(x1.y), lo16(x1.z), hi16(x1.z), lo16(x1.w), hi16(x1.w)};
        float s = 0.f;
#pragma unroll
        for (int k = 0; k < 16; ++k) { v[k] = t[k]; s += t[k]; }
        return wave_sum(s) * (1.f / 1024.f);
    };

    if (roleA) {
        const int kv = xc >> 1, sp = ii * 2 + (xc & 1);
        const size_t kvoff = ((size_t)kv * SK + (SK - WIN) + sp * 16) * 128;
        const rsrc_t rk = mk(pk + kvoff, 7 * 131072 + 4096), rv = mk(pv + kvoff, 7 * 131072 + 4096);
        const int vkv = wave * 131072 + lane * 16;
        v4u kf[4], vf[4];
        wait_until(now() + DLY_KV);
        unsigned xg = ld16(rx, (xc * 128 + wave * 16 + 4 * (lane & 3) + g) * 2);
        const v4u x0 = __builtin_amdgcn_raw_buffer_load_b128(rx, lane * 32, 0, 0), x1 = __builtin_amdgcn_raw_buffer_load_b128(rx, lane * 32 + 16, 0, 0);
#pragma unroll
        for (int t = 0; t < 4; ++t) vf[t] = ldw(rv, vkv, t * 1024);
#pragma unroll
        for (int s = 0; s < 4; ++s) kf[s] = ldw(rk, vkv, s * 1024);
        const rsrc_t rg = mk(pwg, ii < 14 ? 1024 * FF * 2 : 0), ru = mk(pwu, ii < 14 ? 1024 * FF * 2 : 0);
        const int vg = (xc * 128 + wave * 16 + g) * (FF * 2) + (ii * 256 + c16 * 8) * 2;
        ORD();
#pragma unroll
        for (int k = 0; k < NPRE; ++k) wA[k] = ldw(rg, vg, (k >> 1) * FF * 8 + (k & 1) * 256);
        SB();
        int lq = L_Q + wave * 288 + ((lane >> 5) * 64 + c16 * 4 + (g & 1) * 2) * 2, lf = L_Q + (lane & 7) * 288 + g * 16, lm = L_MX + (lane * 8 + wave) * 4, lm2 = L_MX + (lane & 7) * 32;
        int lp = L_P + c16 * 288 + (wave * 16 + g * 4) * 2, po = c16 < 8 ? WS_SO + (kv * 32 + sp) * 4096 + wave * 512 + g * 128 + c16 * 16 : OOB;
        int vo = (g * 4096 + kv * 1024 + wave * 128 + c16 * 4) * 4;
        float xv[16];
        float c0 = rbf(x_mean(x0, x1, xv));
        PIN(c0); PIN(xg); PIN(lq); PIN(lf); PIN(lm); PIN(lm2); PIN(lp); PIN(po); PIN(vo);
#pragma unroll
        for (int t = 0; t < 4; ++t) *(LAS v4u*)(L + L_V + wave * 4096 + t * 1024 + g * 256 + ((c16 ^ ((g << 2) | t)) * 16)) = vf[t];
#pragma unroll
        for (int t = 0; t < 4; ++t) *(LAS v4u*)(L + L_K + wave * 4096 + (4 * t + g) * 256 + ((c16 ^ (4 * t + g)) * 16)) = kf[t];
        BAR();
        {
            const int q4 = c16 >> 2, ch = wave * 2 + ((lane >> 1) & 1);
            const LAS char* a0 = L + L_V + (8 * g + q4) * 256 + ((ch ^ ((q4 << 2) | ((2 * g) & 3))) * 16) + (lane & 1) * 8;
            const LAS char* a1 = L + L_V + (8 * g + 4 + q4) * 256 + ((ch ^ ((q4 << 2) | ((2 * g + 1) & 3))) * 16) + (lane & 1) * 8;
#pragma unroll
            for (int s = 0; s < 4; ++s) kf[s] = *(const LAS v4u*)(L + L_K + wave * 4096 + c16 * 256 + (((4 * s + g) ^ c16) * 16));
#pragma unroll
            for (int s = 0; s < 4; ++s) {
                const v2u lo = __builtin_bit_cast(v2u, __builtin_amdgcn_ds_read_tr16_b64_v4i16((LAS s4*)(a0 + s * 8192)));
                const v2u hi = __builtin_bit_cast(v2u, __builtin_amdgcn_ds_read_tr16_b64_v4i16((LAS s4*)(a1 + s * 8192)));
                vf[s] = v4u{lo.x, lo.y, hi.x, hi.y};
            }
            SB();
        }
        {
            v4u q0, q1;
            SPIN {
                q0 = ldh(ws, vo, WS_Q); q1 = ldh(ws, vo + 256, WS_Q);
                if (all_tagged(df4(q0, T) | df4(q1, T))) break;
                POLL_PAUSE();
            }
            const f4 a = ut4(q0), c = ut4(q1);
            *(LAS unsigned*)(L + lq) = pk2(tp16(tp32(a.x, c.x), tp32(a.z, c.z)), tp16(tp32(a.y, c.y), tp32(a.w, c.w)));
        }
        BAR();
        f4 sc = zero4;
        {
            v4u qf[4];
#pragma unroll
            for (int s = 0; s < 4; ++s) qf[s] = *(const LAS v4u*)(L + lf + s * 64);
            SB();
#pragma unroll
            for (int s = 0; s < 4; ++s) sc = mfma(kf[s], qf[s], sc);
        }
        {
            const unsigned p0 = pk2(sc.x, sc.y), p1 = pk2(sc.z, sc.w);
            const float k = 0.08838834764831845f;
            const unsigned w0 = pk2(lo16(p0) * k, hi16(p0) * k), w1 = pk2(lo16(p1) * k, hi16(p1) * k);
            sc = f4{lo16(w0), hi16(w0), lo16(w1), hi16(w1)};
        }
        const float mw = x32_max(x16_max(mx(mx3(sc.x, sc.y, sc.z), sc.w)));
        if (lane < 8) *(LAS float*)(L + lm) = mw;
        BAR();
        const float l2e = 1.44269504088896f;
        float M;
        {
            const f4 m0 = *(const LAS f4*)(L + lm2), m1 = *(const LAS f4*)(L + lm2 + 16);
            M = mx3(mx3(m0.x, m0.y, m0.z), mx3(m0.w, m1.x, m1.y), mx(m1.z, m1.w)) * l2e;
            const float e0 = __builtin_amdgcn_exp2f(sc.x * l2e - M), e1 = __builtin_amdgcn_exp2f(sc.y * l2e - M), e2 = __builtin_amdgcn_exp2f(sc.z * l2e - M),
                        e3 = __builtin_amdgcn_exp2f(sc.w * l2e - M);
            if (c16 < 8) *(LAS v2u*)(L + lp) = v2u{pk2(e0, e1), pk2(e2, e3)};
        }
        BAR();
        {
            f4 o = zero4;
            v4u pf[4];
#pragma unroll
            for (int s = 0; s < 4; ++s) pf[s] = *(const LAS v4u*)(L + lf + (L_P - L_Q) + s * 64);
            SB();
            f4 ls = zero4;
            const v4u ones = {0x3f803f80u, 0x3f803f80u, 0x3f803f80u, 0x3f803f80u};
#pragma unroll
            for (int s = 0; s < 4; ++s) { ls = mfma(ones, pf[s], ls); o = mfma(vf[s], pf[s], o); }
            const float l = ls.x;
            const unsigned z = asu(M + __builtin_amdgcn_logf(l));
            const float il = __builtin_amdgcn_rcpf(l);
            const unsigned y0 = ((z << 8) & 0xff00u) | T, y1 = (z & 0xff00u) | T, y2 = ((z >> 8) & 0xff00u) | T, y3 = ((z >> 16) & 0xff00u) | T;
            const unsigned p0 = pk2(o.x * il, o.y * il), p1 = pk2(o.z * il, o.w * il);
            const v4u rec = {(p0 << 16) | y0, (p0 & 0xffff0000u) | y1, (p1 << 16) | y2, (p1 & 0xffff0000u) | y3};
            __builtin_amdgcn_raw_buffer_store_b128(rec, ws, po, 0, AUX_SC1);
        }
        ORD();
        if (ii < 14) {
            const int row0 = xc * 128 + wave * 16 + 4 * (lane & 3), p8 = g * 2 + ((lane >> 2) & 1);
            int va = ((lane >> 2) * 1024 + row0) * 4;
            int vmd = (p8 * 4096 + ((lane >> 3) & 1) * 2048 + row0) * 4;
            int vms = (p8 * 4096 + 1024 + row0) * 4;
            int lw = L_RED + ((wave * 2 + (g >> 1)) * 256 + c16 * 8 + (g & 1) * 4) * 4;
            int lw1 = L_RED + lw256, lr1 = L_RED + (wave & 1) * 8192 + lane * 16;
            int lr2 = L_RED + 32768 + (((wave >> 2) * 16 + (g >> 1) * 8 + (g & 1)) * 256 + (wave & 3) * 64 + c16 * 4) * 4;
            const int pg = WS_G + (xc * 3 * 7168 + (wave >> 2) * FF + ii * 256 + (wave & 3) * 64 + c16 * 4) * 4;
            int pg1 = WS_G + (xc * 3 * 7168 + (wave & 1) * FF + ii * 256 + lane * 4) * 4, pg2 = lane < 32 ? pg + (1 + (g & 1)) * 7168 * 4 : OOB;
            PIN(va); PIN(vmd); PIN(vms); PIN(lw); PIN(lw1); PIN(lr1); PIN(lr2); PIN(pg1); PIN(pg2);
            const bool b3 = lane & 8;
            v4u m0u, m1u, a0u, a1u;
#pragma unroll
            for (int k = NPRE; k < 8; ++k) { PACE(k - NPRE, W_G); wA[k] = ldw(rg, vg, (k >> 1) * FF * 8 + (k & 1) * 256); }
#pragma unroll
            for (int k = 0; k < 8; ++k) {
                if (k == MC) { SB(); m0u = ldh(ws, vmd, WS_M2); m1u = ldh(ws, vms, WS_M2); }
                PACE(8 - NPRE + k, k >= MC ? W_G + 2 : W_G);
                wB[k] = ldw(ru, vg, (k >> 1) * FF * 8 + (k & 1) * 256);
            }
            SB();
            if (!all_tagged(df4(m0u, T) | df4(m1u, T))) {
                SPIN {
                    m0u = ldh(ws, vmd, WS_M2); m1u = ldh(ws, vms, WS_M2);
                    if (all_tagged(df4(m0u, T) | df4(m1u, T))) break;
                    POLL_PAUSE();
                }
            }
            float m0 = tp4(ut4(m0u)), m1 = tp4(ut4(m1u));
            m0 = x4_sum(m0); m1 = x4_sum(m1);
            const float mo = dppf<ROR8>(m0);
            float ga = rbf(b3 ? mo : m0), cb1 = rbf(1.f + rbf(b3 ? m0 : mo));
            const float sb = rbf(m1), v2 = b3 ? sb : cb1;
            const unsigned X2 = pk2(v2, v2);
            auto gemv = [&](unsigned X, int lo, int t1, bool half) {
                const v4u A0 = amk<QBC(0)>(X, msk), A1 = amk<QBC(1)>(X, msk);
                f4 c0_ = mfma(A0, wA[0], zero4), c1 = mfma(A0, wA[1], zero4), c2 = mfma(A0, wB[0], zero4), c3 = mfma(A0, wB[1], zero4);
                const v4u A2 = amk<QBC(2)>(X, msk);
                c0_ = mfma(A1, wA[2], c0_); c1 = mfma(A1, wA[3], c1); c2 = mfma(A1, wB[2], c2); c3 = mfma(A1, wB[3], c3);
                const v4u A3 = amk<QBC(3)>(X, msk);
                c0_ = mfma(A2, wA[4], c0_); c1 = mfma(A2, wA[5], c1); c2 = mfma(A2, wB[4], c2); c3 = mfma(A2, wB[5], c3);
                c0_ = mfma(A3, wA[6], c0_); c1 = mfma(A3, wA[7], c1); c2 = mfma(A3, wB[6], c2); c3 = mfma(A3, wB[7], c3);
                LAS f4* r = (LAS f4*)(L + lo);
                if (!half || lane < 32) { r[0] = c0_; r[32] = c1; r[t1] = c2; r[t1 + 32] = c3; }
            };
            {
                const v4u B0 = amk<QBC(0)>(X2, msk), B1 = amk<QBC(1)>(X2, msk), B2 = amk<QBC(2)>(X2, msk), B3 = amk<QBC(3)>(X2, msk);
                LAS f4* r = (LAS f4*)(L + lw + 32768);
                r[0] = mfma(B3, wA[6], mfma(B2, wA[4], mfma(B1, wA[2], mfma(B0, wA[0], zero4)))); r[32] = mfma(B3, wA[7], mfma(B2, wA[5], mfma(B1, wA[3], mfma(B0, wA[1], zero4))));
                f4 c2 = mfma(B1, wB[2], mfma(B0, wB[0], zero4)), c3 = mfma(B1, wB[3], mfma(B0, wB[1], zero4));
                PIN(c2); PIN(c3);
                SB();
                c2 = mfma(B2, wB[4], c2); c3 = mfma(B2, wB[5], c3);
                PIN(c2); PIN(c3);
                SB();
                a0u = ldh(ws, va, WS_O); a1u = ldh(ws, va, WS_O + 65536);
                SB();
                r[1024] = mfma(B3, wB[6], c2); r[1056] = mfma(B3, wB[7], c3);
            }
            BAR();
            {
                const LAS f4* r = (const LAS f4*)(L + lr2);
                pub4(ws, pg2, x32_sum4((r[0] + r[128]) + (r[256] + r[384])), T);
            }
            PIN(ga); PIN(cb1);
            SB();
            if (!all_tagged(df4(a0u, T) | df4(a1u, T))) {
                SPIN {
                    a0u = ldh(ws, va, WS_O); a1u = ldh(ws, va, WS_O + 65536);
                    if (all_tagged(df4(a0u, T) | df4(a1u, T))) break;
                    POLL_PAUSE();
                }
            }
            const float at = q_sum(tp4(ut4(a0u) + ut4(a1u)));
            const float x2 = rbf(asf(xg << 16) + rbf(ga * rbf(at)));
            const float d = x2 - c0, u = d * cb1;
            gemv(pk2(u, u), lw1, 512, true);
            {
                float t = x16_sum(tp32(d, d * d));
                t += dppf<XOR1>(t); t += dppf<XOR2>(t);
                if ((lane & 31) == 0) *(LAS float*)(L + L_MX + (lane >> 5) * 32 + wave * 4) = t;
            }
            BAR();
            if (wave < 2) pub4(ws, pg1, sum8(lr1, 64), T);
            if (wave == 2) {
                const LAS f4* r = (const LAS f4*)(L + L_MX);
                const f4 s = r[0] + r[1], q = r[2] + r[3];
                __builtin_amdgcn_raw_buffer_store_b128(v4u{tg((s.x + s.y) + (s.z + s.w), T), tg((q.x + q.y) + (q.z + q.w), T), E1, ~E1}, ws, lane == 0 ? WS_ST + (ii * 8 + xc) * 128 : OOB, 0, AUX_SC1);
            }
        } else if (wave < 2) {
            const int fi = ((ii - 14) * 8 + xc) * 2 + wave, col = fi * 32 + (lane & 7) * 4, p = lane >> 3;
            const unsigned xq = ld16(rx, (col + g) * 2);
            auto red8 = [&](f4 s) { float t = tp4(s); t += dppf<ROR8>(t); return t; };
            v4u a[4], ga, gb;
            int vd = (p * 1024 + col) * 4, vd3 = p < 4 ? vd : OOB, po = (lane & 8) ? OOB : (col + g) * 2;
            unsigned m3 = p < 4 ? 0xffu : 0u;
            PIN(vd); PIN(vd3); PIN(po); PIN(m3);
            sentinel(ws, (xc * 3 * 7168 + fi * 96) * 4, WS_G + 7168 * 4, T);
            SPIN {
#pragma unroll
                for (int t = 0; t < 4; ++t) a[t] = ldh(ws, vd, WS_O + t * 8 * 4096);
                ga = ldh(ws, (p * 4096 + col) * 4, WS_M2);
                if (all_tagged((df4(a[0], T) | df4(a[1], T)) | (df4(a[2], T) | df4(a[3], T)) | df4(ga, T))) break;
                POLL_PAUSE();
            }
            const float gar = rbf(red8(ut4(ga)));
            float x2 = rbf(asf(xq << 16) + rbf(gar * rbf(red8((ut4(a[0]) + ut4(a[1])) + (ut4(a[2]) + ut4(a[3]))))));
            PIN(x2);
            SPIN {
                gb = ldh(ws, (p * 4096 + col) * 4, WS_M2 + 3072 * 4);
                if (all_tagged(df4(gb, T))) break;
                POLL_PAUSE();
            }
            float gbr = rbf(red8(ut4(gb)));
            PIN(gbr);
            SPIN {
#pragma unroll
                for (int t = 0; t < 3; ++t) a[t] = ldh(ws, vd, WS_D + t * 8 * 4096);
                a[3] = ldh(ws, vd3, WS_D + 3 * 8 * 4096);
                if (all_tagged((df4(a[0], T) | df4(a[1], T)) | (df4(a[2], T) | (df4(a[3], T) & m3)))) break;
                POLL_PAUSE();
            }
            const float y = rbf(red8((ut4(a[0]) + ut4(a[1])) + (ut4(a[2]) + ut4(a[3]))));
            const float o = x2 + rbf(gbr * y);
            __builtin_amdgcn_raw_buffer_store_b16((u16)pk2(o, o), mk(pout, 2048), po, 0, 0);
            if (fi == 0) __builtin_amdgcn_raw_buffer_store_b32(E1, ws, lane == 0 ? WS_EPOCH : OOB, 0, AUX_SC1);
        }
    } else {
        float nrm, c0;
        unsigned cm;
        {
            const int row = (xc >> 1) * 256 + wave * 32 + 4 * (lane & 7) + g;
            const v4u x0 = __builtin_amdgcn_raw_buffer_load_b128(rx, lane * 32, 0, 0), x1 = __builtin_amdgcn_raw_buffer_load_b128(rx, lane * 32 + 16, 0, 0);
            const unsigned xr = ld16(rx, row * 2);
            cm = ld16(rc, (xc * 128 + wave * 16 + 4 * (lane & 3) + g) * 2);
#pragma unroll
            for (int f = 4; f < 8; ++f) wA[f] = ldw(rq, vq, f * 32768);
            SB();
            float v[16];
            const float mean = x_mean(x0, x1, v);
            float q = 0.f;
#pragma unroll
            for (int k = 0; k < 16; ++k) q += (v[k] - mean) * (v[k] - mean);
            const float var = wave_sum(q) * (1.f / 1024.f);
            const float sd = rbf(__builtin_sqrtf(rbf(rbf(var) + 1e-5f)));
            c0 = rbf(mean);
            nrm = rbf(rbf(asf(xr << 16) - c0) / sd);
        }
        int lw = L_RED + (wave * 128 + c16 * 8 + g * 4) * 4, lr = L_RED + (lane & 31) * 16;
        int po = lane < 32 ? WS_Q + ((xc >> 1) * 4096 + (ib * 2 + (xc & 1)) * 128 + lane * 4) * 4 : OOB;
        int vo = ((lane >> 3) * 2048 + (xc >> 1) * 256 + wave * 32 + 4 * (lane & 7)) * 4;
        PIN(nrm); PIN(c0); PIN(cm); PIN(lw); PIN(lr); PIN(po); PIN(vo);
        {
            v4u sa, ca;
            SPIN {
                sa = ldh(ws, vo, WS_M1); ca = ldh(ws, vo, WS_M1 + 4096);
                if (all_tagged(df4(sa, T) | df4(ca, T))) break;
                POLL_PAUSE();
            }
            float st = tp4(ut4(sa)), ct = tp4(ut4(ca));
            st += dppf<ROR8>(st); ct += dppf<ROR8>(ct);
            const unsigned sc = pk2(st, ct);
            const float h = rbf(nrm * rbf(1.f + hi16(sc))) + lo16(sc);
            const unsigned X = pk2(h, h);
            const v4u A0 = amk<BCAST(0)>(X, msk), A1 = amk<BCAST(1)>(X, msk), A2 = amk<BCAST(2)>(X, msk), A3 = amk<BCAST(3)>(X, msk);
            f4 a0 = mfma(A0, wA[0], zero4), a1 = mfma(A1, wA[1], zero4);
            const v4u A4 = amk<BCAST(4)>(X, msk), A5 = amk<BCAST(5)>(X, msk);
            a0 = mfma(A2, wA[2], a0); a1 = mfma(A3, wA[3], a1);
            const v4u A6 = amk<BCAST(6)>(X, msk), A7 = amk<BCAST(7)>(X, msk);
            a0 = mfma(A4, wA[4], a0); a1 = mfma(A5, wA[5], a1);
            a0 = mfma(A6, wA[6], a0); a1 = mfma(A7, wA[7], a1);
            if (lane < 32) *(LAS f4*)(L + lw) = a0 + a1;
            BAR();
            if (wave == 0) {
                pub4(ws, po, sum8(lr, 32), T);
            }
            ORD();
        }
        const u64 t_q = now();
        const int op = xc * 4 + (ib >> 2);
        const int rs = (xc >> 1) * 7 + (ib >> 1), cd = (ib & 1) * 2 + (xc & 1);
        const rsrc_t rd = mk(pwd, ib < 14 ? FF * 1024 * 2 : 0);
        const int vd = (rs * 128 + wave * 16 + g) * 2048 + (cd * 256 + c16 * 8) * 2;
        const bool m2late = ib >= 12;
        const int vm = (xc * 128 + wave * 16 + g) * 12288 + (2048 + cm2 * 256 + c16 * 8) * 2;
        auto phase_m2 = [&] {
            const float sm = silu(asf(cm << 16));
            const unsigned XM = pk2(sm, sm);
            f4 a0 = zero4, a1 = zero4;
            { const v4u A = amk<QBC(0)>(XM, msk); a0 = mfma(A, wA[0], a0); a1 = mfma(A, wA[1], a1); }
            { const v4u A = amk<QBC(1)>(XM, msk); a0 = mfma(A, wA[2], a0); a1 = mfma(A, wA[3], a1); }
            { const v4u A = amk<QBC(2)>(XM, msk); a0 = mfma(A, wA[4], a0); a1 = mfma(A, wA[5], a1); }
            { const v4u A = amk<QBC(3)>(XM, msk); a0 = mfma(A, wA[6], a0); a1 = mfma(A, wA[7], a1); }
            reduce256(a0, a1, L_K + lw256, L_K + lr256, WS_M2 + (xc * 4096 + cm2 * 256) * 4 + po256);
        };
        {
            const rsrc_t ro = mk(pwo, 4096 * 1024 * 2), rm1 = mk(pwm, m2late ? 0 : 1024 * 6144 * 2);
            const int vw = (((op & 24) + (wave & 1) * 4) * 128 + (op & 7) * 16 + (wave >> 1) * 4 + g) * 2048 + (co * 256 + c16 * 8) * 2;
#pragma unroll
            for (int k = 0; k < 8; ++k) wA[k] = ldw(rm1, vm, (k >> 1) * 49152 + (k & 1) * 256);
            ORD();
#pragma unroll
            for (int k = 0; k < 8; ++k) wB[k] = ldw(ro, vw, (k >> 1) * 262144 + (k & 1) * 256);
            SB();
            if (!m2late) phase_m2();
        }
        {
            int vs = ((op >> 3) * 32 + (lane >> 2)) * 4096 + (op & 7) * 512 + wave * 64 + (lane & 3) * 16;
            int lw2 = L_V + lw256, lr2 = L_V + lr256, po2 = WS_O + (op * 1024 + co * 256) * 4 + po256;
            PIN(vs); PIN(lw2); PIN(lr2); PIN(po2);
            wait_until(t_q + DLY_S);
            v4u o0, o1;
            SPIN {
                o0 = ldh(ws, vs, WS_SO); o1 = ldh(ws, vs, WS_SO + 65536);
                if (all_tagged(df4(o0, T) | df4(o1, T))) break;
                POLL_PAUSE();
            }
            const float z0 = asf(__builtin_amdgcn_perm(__builtin_amdgcn_perm(o0.w, o0.z, 0x0c0c0501u), __builtin_amdgcn_perm(o0.y, o0.x, 0x0c0c0501u), 0x05040100u));
            const float z1 = asf(__builtin_amdgcn_perm(__builtin_amdgcn_perm(o1.w, o1.z, 0x0c0c0501u), __builtin_amdgcn_perm(o1.y, o1.x, 0x0c0c0501u), 0x05040100u));
            float Z = mx(z0, z1);
            asm("s_nop 1\n\tv_max_f32_dpp %0, %0, %0 row_ror:4 row_mask:0xf bank_mask:0xf\n\ts_nop 1\n\tv_max_f32_dpp %0, %0, %0 row_ror:8 row_mask:0xf bank_mask:0xf" : "+v"(Z));
            Z = x32_max(x16_max(Z));
            const float w0 = __builtin_amdgcn_exp2f(z0 - Z), w1 = __builtin_amdgcn_exp2f(z1 - Z);
            const float Ls = x32_sum(x16_sum(q_sum(w0 + w1)));
            float t = q_sum(tp4(f4{hi16(o0.x), hi16(o0.y), hi16(o0.z), hi16(o0.w)} * w0 + f4{hi16(o1.x), hi16(o1.y), hi16(o1.z), hi16(o1.w)} * w1));
            t = t * __builtin_amdgcn_rcpf(Ls);
            const unsigned X = pk2(t, t);
            const v4u A0 = amk<QBC(0)>(X, msk), A1 = amk<QBC(1)>(X, msk);
            f4 a0 = mfma(A0, wB[0], zero4), a1 = mfma(A0, wB[1], zero4);
            const v4u A2 = amk<QBC(2)>(X, msk);
            a0 = mfma(A1, wB[2], a0); a1 = mfma(A1, wB[3], a1);
            const v4u A3 = amk<QBC(3)>(X, msk);
            a0 = mfma(A2, wB[4], a0); a1 = mfma(A2, wB[5], a1);
            a0 = mfma(A3, wB[6], a0); a1 = mfma(A3, wB[7], a1);
            reduce256(a0, a1, lw2, lr2, po2);
            ORD();
        }
        if (ib >= 14) {
            const int vb = (xc * 128 + wave * 16 + g) * 12288 + (5120 + (ib - 14) * 512 + c16 * 8) * 2;
#pragma unroll
            for (int k = 0; k < 8; ++k) wA[k] = ldw(rm, vb, (k >> 1) * 49152 + (k & 1) * 256);
#pragma unroll
            for (int k = 0; k < 8; ++k) wC[k] = ldw(rm, vb, (k >> 1) * 49152 + (k & 1) * 256 + 512);
            SB();
            const float sg = silu(asf(cm << 16));
            const unsigned XG = pk2(sg, sg);
            const v4u A0 = amk<QBC(0)>(XG, msk), A1 = amk<QBC(1)>(XG, msk), A2 = amk<QBC(2)>(XG, msk), A3 = amk<QBC(3)>(XG, msk);
            const f4 c0_ = mfma(A3, wA[6], mfma(A2, wA[4], mfma(A1, wA[2], mfma(A0, wA[0], zero4)))), c1 = mfma(A3, wA[7], mfma(A2, wA[5], mfma(A1, wA[3], mfma(A0, wA[1], zero4))));
            const f4 c2 = mfma(A3, wC[6], mfma(A2, wC[4], mfma(A1, wC[2], mfma(A0, wC[0], zero4)))), c3 = mfma(A3, wC[7], mfma(A2, wC[5], mfma(A1, wC[3], mfma(A0, wC[1], zero4))));
            if (lane < 32) { LAS f4* r = (LAS f4*)(L + L_RED + lw256); r[0] = c0_; r[32] = c1; r[512] = c2; r[544] = c3; }
            BAR();
            if (wave < 2) pub4(ws, WS_M2 + (xc * 4096 + 3072 + (ib - 14) * 512 + wave * 256 + lane * 4) * 4, sum8(L_RED + wave * 8192 + lane * 16, 64), T);
        } else {
            wait_until(now() + DLY_D);
#pragma unroll
            for (int k = 0; k < 8; ++k) wC[k] = ldw(rd, vd, (k >> 1) * 8192 + (k & 1) * 256);
            SB();
            int vo = ((g * 2 + ((lane >> 2) & 1)) * 3 * 7168 + ((lane >> 3) & 1) * FF + rs * 128 + wave * 16 + 4 * (lane & 3)) * 4;
            int lw2 = L_RED + lw256, lr2 = L_RED + lr256, po2 = WS_D + (rs * 1024 + cd * 256) * 4 + po256;
            PIN(vo); PIN(lw2); PIN(lr2); PIN(po2);
            int vst = ((rs >> 1) * 8 + (lane >> 3)) * 128;
            PIN(vst);
            v4u st, tA, tB, tC;
            sentinel(ws, vo, WS_G + 7168 * 4, T);
            SPIN {
                tB = ldh(ws, vo, WS_G + 7168 * 4); tC = ldh(ws, vo, WS_G + 2 * 7168 * 4);
                if (all_tagged(df4(tB, T) | df4(tC, T))) break;
                POLL_PAUSE();
            }
            float sB = tp4(ut4(tB)), sC = tp4(ut4(tC));
            sB = x4_sum(sB); sC = x4_sum(sC);
            PIN(sB); PIN(sC);
            SPIN {
                st = ldh(ws, vst, WS_ST); tA = ldh(ws, vo, WS_G);
                if (__builtin_amdgcn_ballot_w64((((ne3(st.x, st.y, T) | df4(tA, T)) & 0xffu) | (st.z ^ E1) | (st.w ^ ~E1)) != 0u) == 0ull) break;
                POLL_PAUSE();
            }
            float t = tp4(ut4(tA));
            float ss = x16_sum(tp32(ut(st.x), ut(st.y)));
            ss += dppf<ROR8>(ss);
            t = x4_sum(t);
            const float dm = asf(__builtin_amdgcn_readlane(asu(ss), 0)) * (1.f / 1024.f), q2 = asf(__builtin_amdgcn_readlane(asu(ss), 32)) * (1.f / 1024.f);
            const float rstd = __builtin_amdgcn_rcpf(rbf(__builtin_amdgcn_sqrtf(rbf(rbf(q2 - dm * dm) + 1e-5f))));
            t = rbf((t - (rbf(c0 + dm) - c0) * sB) * rstd + sC);
            const float act = rbf(silu(t)) * dppf<ROR8>(t);
            const unsigned X = pk2(act, act);
            const v4u A0 = amk<QBC(0)>(X, msk), A1 = amk<QBC(1)>(X, msk);
            f4 a0 = mfma(A0, wC[0], zero4), a1 = mfma(A0, wC[1], zero4);
            const v4u A2 = amk<QBC(2)>(X, msk);
            a0 = mfma(A1, wC[2], a0); a1 = mfma(A1, wC[3], a1);
            const v4u A3 = amk<QBC(3)>(X, msk);
            a0 = mfma(A2, wC[4], a0); a1 = mfma(A2, wC[5], a1);
            a0 = mfma(A3, wC[6], a0); a1 = mfma(A3, wC[7], a1);
            reduce256(a0, a1, lw2, lr2, po2);
        }
    }
}

extern "C" void kernel_launch(void* const* d_in, const int* in_sizes, int n_in, void* d_out, int out_size, void* d_ws, size_t ws_size, hipStream_t stream) {
    fused_block<<<256, 512, 0, stream>>>((const u16*)d_in[0], (const u16*)d_in[1], (const u16*)d_in[2], (const u16*)d_in[3], (const u16*)d_in[4], (const u16*)d_in[6],
                                         (const u16*)d_in[7], (const u16*)d_in[8], (const u16*)d_in[9], (const u16*)d_in[10], (u16*)d_out, (char*)d_ws);
}
```
